```python
import torch

def _softmax(x, axis=-1):
    return torch.softmax(x, dim=axis)
def _gelu(x):
    return torch.nn.functional.gelu(x, approximate="tanh")
def _silu(x):
    return torch.nn.functional.silu(x)
def _sigmoid(x):
    return torch.sigmoid(x)
def _layernorm(x, axis=-1, eps=1e-5):
    m = x.mean(dim=axis, keepdim=True)
    v = x.var(dim=axis, keepdim=True, unbiased=False)
    return (x - m) / torch.sqrt(v + eps)
def _scatter_add(tgt, idx, src, axis=0):
    return tgt.clone().index_add_(axis, idx, src)
def _segment_sum(src, seg, n):
    out = torch.zeros((n,) + src.shape[1:], dtype=src.dtype)
    return out.index_add_(0, seg, src)
def _pad(x, pw):
    flat = tuple(v for pair in reversed(pw) for v in pair)
    return torch.nn.functional.pad(x, flat)

kernel_name = 'standalone__paged_block_table_kv_pagedattention__decode'

def setup_inputs():

    _w = (lambda a: (lambda t: t.to(torch.bfloat16) if t.is_floating_point() else t)(torch.as_tensor(a)))
    B, Hq, Hkv, Sq, Skv, D = 2, 32, 32, 1, 8192, 128
    bs = 16
    nbps = max(1, Skv // bs)
    num_phys = B * nbps + 4
    bt = torch.randperm(num_phys)[: B * nbps].reshape(B, nbps).to(torch.int64)
    cl = (nbps * bs - (torch.arange(1, B + 1, dtype=torch.int64) * 2654435761) % max(1, nbps * bs // 2)).to(torch.int64)
    return {
        "q": _w((lambda *s: torch.randn(*s, dtype=torch.bfloat16) * 0.8)(B, Hq, Sq, D)),
        "k_cache": _w((lambda *s: torch.randn(*s, dtype=torch.bfloat16) * 0.8)(num_phys, bs, Hkv, D)),
        "v_cache": _w((lambda *s: torch.randn(*s, dtype=torch.bfloat16) * 0.05)(num_phys, bs, Hkv, D)),
        "block_table": _w(bt),
        "context_lens": _w(cl),
    }

def reference(q, k_cache, v_cache, block_table, context_lens):
    B, Hq, Sq, D = q.shape
    bs = k_cache.shape[1]
    Hkv = k_cache.shape[2]
    nbps = block_table.shape[1]
    Skv = nbps * bs
    bt = block_table.reshape(-1)
    k = (lambda x, *d: x.permute(*d))(k_cache[bt].reshape(B, Skv, Hkv, D), 0, 2, 1, 3)
    v = (lambda x, *d: x.permute(*d))(v_cache[bt].reshape(B, Skv, Hkv, D), 0, 2, 1, 3)
    g = Hq // Hkv
    qg = q.reshape(B, Hkv, g, Sq, D)
    scores = torch.einsum("bhgqd,bhkd->bhgqk", qg, k) * (D ** -0.5)
    pos = torch.arange(Skv)
    msk = pos[None, :] < context_lens[:, None]
    scores = torch.where(msk[:, None, None, None, :], scores, -1e9)
    p = _softmax(scores, axis=-1)
    out = torch.einsum("bhgqk,bhkd->bhgqd", p, v)
    return out.reshape(B, Hq, Sq, D)
```

```cpp
#include <hip/hip_runtime.h>
#include <hip/hip_bf16.h>
#include <stdint.h>

#define NSW 7
#define CH 2
#define AUX 2
#define TAIL_SEQ 1
#define TAILN 4
#define ST_AUX 16
#define CHEAP_SLEEP 4
#define SPIN_CAP (1 << 20)

constexpr int B_ = 2, H_ = 32, D_ = 128, PAGE = 16, NBPS = 512, SKV = NBPS * PAGE;
constexpr int NWG = 256, NHQ = 8, WGPG = NWG / NHQ;
constexpr int NWAVES = 8, NTHR = NWAVES * 64;
constexpr unsigned TOK_BYTES = H_ * D_ * 2;
constexpr unsigned PAGE_BYTES = PAGE * TOK_BYTES;
static_assert(PAGE % CH == 0 && NSW <= NWAVES, "geometry");

constexpr float SCALE = 0.088388347648318440f;
constexpr float C2 = SCALE * 1.4426950408889634f;
constexpr float NEG_BIG = -3.0e38f;

using u32x4 = __attribute__((ext_vector_type(4))) unsigned;
using u32x2 = __attribute__((ext_vector_type(2))) unsigned;
using f32x4 = __attribute__((ext_vector_type(4))) float;
using f32x2 = __attribute__((ext_vector_type(2))) float;
using bf16x2 = __attribute__((ext_vector_type(2))) __bf16;
typedef unsigned long long u64;

__device__ __forceinline__ float asf(unsigned x) { return __builtin_bit_cast(float, x); }
__device__ __forceinline__ unsigned asu(float x) { return __builtin_bit_cast(unsigned, x); }
__device__ __forceinline__ float dot2(unsigned k, unsigned q, float acc) {
  return __builtin_amdgcn_fdot2_f32_bf16(__builtin_bit_cast(bf16x2, k), __builtin_bit_cast(bf16x2, q), acc, false);
}
template <int CTRL> __device__ __forceinline__ float dpp(float x) {
  return asf(__builtin_amdgcn_mov_dpp(__builtin_bit_cast(int, x), CTRL, 0xf, 0xf, true));
}
__device__ __forceinline__ float row_sum(float x) {
  x += dpp<0x128>(x); x += dpp<0x124>(x); x += dpp<0x122>(x); x += dpp<0x121>(x);
  return x;
}
__device__ __forceinline__ float row_max(float x) {
  x = fmaxf(x, dpp<0x128>(x)); x = fmaxf(x, dpp<0x124>(x)); x = fmaxf(x, dpp<0x122>(x)); x = fmaxf(x, dpp<0x121>(x));
  return x;
}
__device__ __forceinline__ float xrow_max(float x) {
  auto a = __builtin_amdgcn_permlane16_swap(asu(x), asu(x), false, false);
  x = fmaxf(asf(a[0]), asf(a[1]));
  auto c = __builtin_amdgcn_permlane32_swap(asu(x), asu(x), false, false);
  return fmaxf(asf(c[0]), asf(c[1]));
}
__device__ __forceinline__ float xrow_sum(float x) {
  auto a = __builtin_amdgcn_permlane16_swap(asu(x), asu(x), false, false);
  x = asf(a[0]) + asf(a[1]);
  auto c = __builtin_amdgcn_permlane32_swap(asu(x), asu(x), false, false);
  return asf(c[0]) + asf(c[1]);
}
__device__ __forceinline__ int clamp_len(int l) { return l < 0 ? 0 : (l > SKV ? SKV : l); }

constexpr unsigned WS_CTL = 0, WS_GM = 64 * 1024, WS_GO = 128 * 1024, WS_BYTES = WS_GO + B_ * NHQ * 8 * WGPG * 512;
constexpr unsigned OOB = 0x80000000u;

constexpr int LDS_ACC = 0, LDS_ML = NSW * 512, LDS_BT = LDS_ML + NSW * 8, LDS_FLOATS = LDS_BT + B_ * NBPS;

__global__ void __launch_bounds__(NTHR) attn_decode(const unsigned short* __restrict__ q, const unsigned short* __restrict__ kc,
                                                    const unsigned short* __restrict__ vc, const int* __restrict__ bt,
                                                    const int* __restrict__ lens, unsigned char* __restrict__ ws,
                                                    unsigned short* __restrict__ out, unsigned kv_bytes) {
  __shared__ __attribute__((aligned(16))) float lds[LDS_FLOATS];
  int* lbt = reinterpret_cast<int*>(lds + LDS_BT);
  asm volatile("" :: "s"(q), "s"(kc), "s"(vc), "s"(bt), "s"(lens), "s"(ws), "s"(out), "s"(kv_bytes));
  const int tid = threadIdx.x, lane = tid & 63;
  const int wave = __builtin_amdgcn_readfirstlane(tid >> 6);
  const int hq = (blockIdx.x >> 3) & 7, j = (blockIdx.x & 7) + 8 * (blockIdx.x >> 6);

  u32x2 lr = *reinterpret_cast<const u32x2*>(lens);
  u32x4 btq[4];
#pragma unroll
  for (int i = 0; i < 4; ++i) btq[i] = reinterpret_cast<const u32x4*>(bt)[i * 64 + lane];
  __builtin_amdgcn_sched_barrier(0);
  const unsigned voff = hq * 1024 + lane * 16;
  const __amdgpu_buffer_rsrc_t wrs = __builtin_amdgcn_make_buffer_rsrc((void*)ws, 0, WS_BYTES, 0x00020000);
  const u32x4 qa = *reinterpret_cast<const u32x4*>(reinterpret_cast<const char*>(q) + voff);
  const u32x4 qb = *reinterpret_cast<const u32x4*>(reinterpret_cast<const char*>(q) + voff + H_ * D_ * 2);
  const u32x4 cw = __builtin_amdgcn_raw_buffer_load_b128(wrs, 0, WS_CTL + hq * 256u, 16);
  const __amdgpu_buffer_rsrc_t krs = __builtin_amdgcn_make_buffer_rsrc((void*)kc, 0, kv_bytes, 0x00020000);
  const __amdgpu_buffer_rsrc_t vrs = __builtin_amdgcn_make_buffer_rsrc((void*)vc, 0, kv_bytes, 0x00020000);
  __builtin_amdgcn_sched_barrier(0);
  asm volatile("" : "+v"(lr));
  const int len0 = clamp_len(__builtin_amdgcn_readfirstlane(lr[0])), len1 = clamp_len(__builtin_amdgcn_readfirstlane(lr[1]));
  int n0 = __builtin_popcount((unsigned)__builtin_amdgcn_ballot_w64((2 * lane + 1) * (len0 + len1) <= 2 * WGPG * len0));
  n0 = min(max(n0, 1), WGPG - 1);
  const int b = j < n0 ? 0 : 1;
  const int s = b ? j - n0 : j, n = b ? WGPG - n0 : n0, len = b ? len1 : len0;
  const float rn = __builtin_amdgcn_rcpf((float)n);
  const int t0 = __builtin_amdgcn_readfirstlane((int)((float)(len * s) * rn));
  const int t1 = (s + 1 == n) ? len : __builtin_amdgcn_readfirstlane((int)((float)(len * (s + 1)) * rn));
  int tb = (t0 & ~(CH - 1)) + wave * CH;
#pragma unroll
  for (int i = 0; i < 4; ++i) reinterpret_cast<u32x4*>(lbt)[i * 64 + lane] = btq[i];
  asm volatile("" ::: "memory");
  const int* lbtb = lbt + b * NBPS;
  float m = NEG_BIG, l = 0.f;
  f32x2 acc[4];
#pragma unroll
  for (int k = 0; k < 4; ++k) acc[k] = f32x2{0.f, 0.f};
  constexpr int STEP = NSW * CH;
  const int tend = wave < NSW ? t1 : 0;
  const int tsw = tend - TAILN * STEP;
#define ISSUE(K_, V_, tb_, pg_)                                                                            \
  do {                                                                                                     \
    const unsigned so_ = (unsigned)(pg_) * PAGE_BYTES + (unsigned)((tb_) & 15) * TOK_BYTES;                \
    _Pragma("unroll") for (int u_ = 0; u_ < CH; ++u_) {                                                    \
      const unsigned o_ = ((tb_) + u_ >= t0 && (tb_) + u_ < tend) ? so_ + u_ * TOK_BYTES : kv_bytes;       \
      (K_)[u_] = __builtin_amdgcn_raw_buffer_load_b128(krs, voff, o_, AUX);                                \
      (V_)[u_] = __builtin_amdgcn_raw_buffer_load_b128(vrs, voff, o_, AUX);                                \
    }                                                                                                      \
  } while (0)
#define CONSUME(K_, V_, NC_)                                                                               \
  do {                                                                                                     \
    float sco_[(NC_) * CH];                                                                                \
    _Pragma("unroll") for (int u_ = 0; u_ < (NC_) * CH; ++u_) {                                            \
      const int t_ = tb + (u_ / CH) * STEP + u_ % CH;                                                      \
      float d_ = (t_ >= t0 && t_ < tend) ? 0.f : -INFINITY;                                                \
      const u32x4 kk_ = (K_)[u_];                                                                          \
      d_ = dot2(kk_[0], qv[0], d_); d_ = dot2(kk_[1], qv[1], d_); d_ = dot2(kk_[2], qv[2], d_); d_ = dot2(kk_[3], qv[3], d_); \
      sco_[u_] = row_sum(d_);                                                                              \
    }                                                                                                      \
    float mn_ = m;                                                                                         \
    _Pragma("unroll") for (int u_ = 0; u_ < (NC_) * CH; ++u_) mn_ = fmaxf(mn_, sco_[u_]);                  \
    const float alpha_ = __builtin_amdgcn_exp2f((m - mn_) * C2);                                           \
    const float mc_ = -mn_ * C2;                                                                           \
    m = mn_;                                                                                               \
    l *= alpha_;                                                                                           \
    _Pragma("unroll") for (int k_ = 0; k_ < 4; ++k_) acc[k_] *= alpha_;                                    \
    _Pragma("unroll") for (int u_ = 0; u_ < (NC_) * CH; ++u_) {                                            \
      const float p_ = __builtin_amdgcn_exp2f(fmaf(sco_[u_], C2, mc_));                                    \
      const u32x4 vv_ = (V_)[u_];                                                                          \
      l += p_;                                                                                             \
      _Pragma("unroll") for (int k_ = 0; k_ < 4; ++k_) {                                                   \
        const f32x2 vf_ = {asf(vv_[k_] << 16), asf(vv_[k_] & 0xffff0000u)};                               \
        acc[k_] = __builtin_elementwise_fma(f32x2{p_, p_}, vf_, acc[k_]);                                  \
      }                                                                                                    \
    }                                                                                                      \
    asm volatile("" : "+v"(acc[0]), "+v"(acc[1]), "+v"(acc[2]), "+v"(acc[3]), "+v"(l), "+v"(m));           \
  } while (0)
  u32x4 K[CH], V[CH];
  __builtin_amdgcn_sched_barrier(0);
  ISSUE(K, V, tb, __builtin_amdgcn_readfirstlane(lbtb[min(tb >> 4, NBPS - 1)]));
  __builtin_amdgcn_sched_barrier(0);
  const u64 epoch = b ? ((u64)(unsigned)__builtin_amdgcn_readfirstlane(cw[3]) << 32 | (unsigned)__builtin_amdgcn_readfirstlane(cw[2]))
                      : ((u64)(unsigned)__builtin_amdgcn_readfirstlane(cw[1]) << 32 | (unsigned)__builtin_amdgcn_readfirstlane(cw[0]));
  unsigned tag = (unsigned)(((epoch + 1) * 0x9E3779B97F4A7C15ull) >> 32);
  asm volatile("" : "+s"(tag));
  const u32x4 qv = b ? qb : qa;
  for (;;) {
    const int pgv = lbtb[min((tb + STEP) >> 4, NBPS - 1)];
    CONSUME(K, V, 1);
    __builtin_amdgcn_sched_barrier(0);
    tb += STEP;
    asm volatile("" : "+s"(tb));
    if (tb >= tsw) break;
    ISSUE(K, V, tb, __builtin_amdgcn_readfirstlane(pgv));
    __builtin_amdgcn_sched_barrier(0);
  }
  {
    u32x4 KB[TAILN * CH], VB[TAILN * CH];
    int pgs[TAILN];
#pragma unroll
    for (int c = 0; c < TAILN; ++c) pgs[c] = lbtb[min((tb + c * STEP) >> 4, NBPS - 1)];
#pragma unroll
    for (int c = 0; c < TAILN; ++c) ISSUE(KB + c * CH, VB + c * CH, tb + c * STEP, __builtin_amdgcn_readfirstlane(pgs[c]));
    __builtin_amdgcn_sched_barrier(0);
#pragma unroll
    for (int c = 0; c < TAILN; ++c) { CONSUME(KB + c * CH, VB + c * CH, 1); __builtin_amdgcn_sched_barrier(0); tb += STEP; }
  }
#undef ISSUE
#undef CONSUME
  const int grp = b * NHQ + hq;
  float* lacc = lds + LDS_ACC;
  float* lml = lds + LDS_ML;
  if (wave < NSW) {
    *reinterpret_cast<f32x4*>(lacc + (wave * 64 + lane) * 8) = f32x4{acc[0][0], acc[0][1], acc[1][0], acc[1][1]};
    *reinterpret_cast<f32x4*>(lacc + (wave * 64 + lane) * 8 + 4) = f32x4{acc[2][0], acc[2][1], acc[3][0], acc[3][1]};
    if ((lane & 15) == 0) *reinterpret_cast<f32x2*>(lml + (wave * 4 + (lane >> 4)) * 2) = f32x2{m, l};
  }
  asm volatile("s_waitcnt lgkmcnt(0)\n\ts_barrier" ::: "memory");
  if (wave < 4) {
    f32x2 ml[NSW];
    float x0[NSW], x1[NSW];
#pragma unroll
    for (int w = 0; w < NSW; ++w) {
      ml[w] = *reinterpret_cast<const f32x2*>(lml + (w * 4 + wave) * 2);
      x0[w] = lacc[(w * 64 + wave * 16) * 8 + lane];
      x1[w] = lacc[(w * 64 + wave * 16) * 8 + 64 + lane];
    }
    float M = ml[0][0];
#pragma unroll
    for (int w = 1; w < NSW; ++w) M = fmaxf(M, ml[w][0]);
    float L = 0.f, o0 = 0.f, o1 = 0.f;
#pragma unroll
    for (int w = 0; w < NSW; ++w) {
      const float wgt = __builtin_amdgcn_exp2f((ml[w][0] - M) * C2);
      L = fmaf(wgt, ml[w][1], L); o0 = fmaf(wgt, x0[w], o0); o1 = fmaf(wgt, x1[w], o1);
    }
    const unsigned gso = WS_GO + (unsigned)((grp * 8 + wave * 2) * WGPG + s) * 512u;
    __builtin_amdgcn_raw_buffer_store_b64(u32x2{asu(o0), tag}, wrs, lane * 8, gso, ST_AUX);
    __builtin_amdgcn_raw_buffer_store_b64(u32x2{asu(o1), tag}, wrs, lane * 8, gso + WGPG * 512u, ST_AUX);
    __builtin_amdgcn_raw_buffer_store_b64(u32x2{asu((lane & 1) ? L : M), tag}, wrs, lane < 2 ? lane * 8 : OOB,
                                          WS_GM + (unsigned)((grp * 4 + wave) * WGPG + s) * 16u, ST_AUX);
    return;
  }
  const int g = wave - 4, r = lane >> 4, c = lane & 15;
  const int msrc = (c & 7) * 4 + r;
  const unsigned moff = msrc * 16 + (c >> 3) * 8, mwant = msrc < n ? tag : 0u;
  const unsigned ooff = r * 512 + (g * 16 + c) * 8;
  unsigned want[8];
#pragma unroll
  for (int i = 0; i < 8; ++i) want[i] = 4 * i + r < n ? tag : 0u;
#pragma nounroll
  for (int k = s; k < 8; k += n) {
    const int hr = k >> 1, half = k & 1;
    const __amdgpu_buffer_rsrc_t mrs = __builtin_amdgcn_make_buffer_rsrc(ws + WS_GM + (unsigned)((grp * 4 + hr) * WGPG) * 16u, 0, n * 16, 0x00020000);
    unsigned char* obase = ws + WS_GO + (unsigned)((grp * 8 + k) * WGPG) * 512u;
    u32x2 gm, go[8];
#define POLL_LOAD(GM_, GO_, FULL_)                                                                                          \
  do {                                                                                                                      \
    asm volatile("" ::: "memory");                                                                                          \
    const __amdgpu_buffer_rsrc_t grs_ = __builtin_amdgcn_make_buffer_rsrc(obase, 0, (FULL_) ? n * 512 : 0, 0x00020000);     \
    GM_ = __builtin_amdgcn_raw_buffer_load_b64(mrs, moff, 0, 16);                                                           \
    _Pragma("unroll") for (int i_ = 0; i_ < 8; ++i_) GO_[i_] = __builtin_amdgcn_raw_buffer_load_b64(grs_, ooff, i_ * 2048, 16); \
  } while (0)
#define POLL_CHECK(GM_, GO_, FULL_, DONE_, NEXT_FULL_)                                                                      \
  do {                                                                                                                      \
    const int miss_ = __builtin_popcountll(__builtin_amdgcn_ballot_w64(GM_[1] != mwant));                                   \
    unsigned bad_ = 0u;                                                                                                     \
    _Pragma("unroll") for (int i_ = 0; i_ < 8; ++i_) bad_ |= GO_[i_][1] ^ want[i_];                                         \
    DONE_ = (FULL_) && miss_ == 0 && __builtin_amdgcn_ballot_w64(bad_ != 0u) == 0ull;                                       \
    NEXT_FULL_ = miss_ <= n;                                                                                                \
  } while (0)
    bool full = true, done = false;
#pragma nounroll
    for (int spin = 0; spin < SPIN_CAP; ++spin) {
      POLL_LOAD(gm, go, full);
      bool nf;
      POLL_CHECK(gm, go, full, done, nf);
      if (done) break;
      full = nf;
      if (!full) __builtin_amdgcn_s_sleep(CHEAP_SLEEP);
    }
    const float mv = (c < 8 && msrc < n) ? asf(gm[0]) : NEG_BIG;
    const float GM = xrow_max(row_max(mv));
    const float wv = __builtin_amdgcn_exp2f((mv - GM) * C2);
    float o = dpp<0x150>(wv) * asf(go[0][0]);
    o = fmaf(dpp<0x151>(wv), asf(go[1][0]), o); o = fmaf(dpp<0x152>(wv), asf(go[2][0]), o); o = fmaf(dpp<0x153>(wv), asf(go[3][0]), o);
    o = fmaf(dpp<0x154>(wv), asf(go[4][0]), o); o = fmaf(dpp<0x155>(wv), asf(go[5][0]), o); o = fmaf(dpp<0x156>(wv), asf(go[6][0]), o);
    o = fmaf(dpp<0x157>(wv), asf(go[7][0]), o);
    const float wl = dpp<0x128>(wv);
    float GL = xrow_sum(row_sum(c >= 8 ? wl * asf(gm[0]) : 0.f));
    float GO = xrow_sum(o);
    const int head = hq * 4 + hr, d = half * 64 + g * 16 + c;
    constexpr float MASK_RAW = -998244352.0f / SCALE;
    if (len < SKV && (GM - MASK_RAW) * C2 < 160.f) {
      const float M2 = fmaxf(GM, MASK_RAW);
      const float w0 = (GL > 0.f) ? __builtin_amdgcn_exp2f((GM - M2) * C2) : 0.f;
      const float wm = __builtin_amdgcn_exp2f((MASK_RAW - M2) * C2);
      float sv = 0.f;
      for (int t = len + r; t < SKV; t += 4)
        sv += asf((unsigned)vc[(((size_t)lbtb[t >> 4] * PAGE + (t & 15)) * H_ + head) * D_ + d] << 16);
      sv = xrow_sum(sv);
      GL = GL * w0 + wm * (float)(SKV - len);
      GO = GO * w0 + wm * sv;
    }
    if (r == 0) out[(b * H_ + head) * D_ + d] = __builtin_bit_cast(unsigned short, (__bf16)(GO * __builtin_amdgcn_rcpf(GL)));
    if (k == 0 && g == 0)
      __builtin_amdgcn_raw_buffer_store_b64(u32x2{(unsigned)(epoch + 1), (unsigned)((epoch + 1) >> 32)}, wrs,
                                            lane == 0 ? b * 8u : OOB, WS_CTL + hq * 256u, 16);
  }
}

extern "C" void kernel_launch(void* const* d_in, const int* in_sizes, int n_in, void* d_out, int out_size, void* d_ws,
                              size_t ws_size, hipStream_t stream) {
  const unsigned short* q = (const unsigned short*)d_in[0];
  const unsigned short* k = (const unsigned short*)d_in[1];
  const unsigned short* v = (const unsigned short*)d_in[2];
  const int* bt = (const int*)d_in[3];
  const int* lens = (const int*)d_in[4];
  const unsigned kv_bytes = (unsigned)in_sizes[1] * 2u;
  attn_decode<<<NWG, NTHR, 0, stream>>>(q, k, v, bt, lens, (unsigned char*)d_ws, (unsigned short*)d_out, kv_bytes);
}
```

```cpp
#include <hip/hip_runtime.h>
#include <hip/hip_bf16.h>
#include <stdint.h>

constexpr int NSW = 7;
constexpr int CH = 2;
constexpr int TAILN = 4;
constexpr int AUX_NT = 2;
constexpr int AUX_SC1 = 16;
constexpr int SPIN_CAP = 1 << 20;

constexpr int B_ = 2, H_ = 32, D_ = 128, PAGE = 16, NBPS = 512, SKV = NBPS * PAGE;
constexpr int NWG = 256, NHQ = 8, WGPG = NWG / NHQ;
constexpr int NWAVES = 8, NTHR = NWAVES * 64;
constexpr unsigned TOK_BYTES = H_ * D_ * 2;
constexpr unsigned PAGE_BYTES = PAGE * TOK_BYTES;
static_assert(PAGE % CH == 0 && NSW <= NWAVES, "geometry");

constexpr float SCALE = 0.088388347648318440f;
constexpr float C2 = SCALE * 1.4426950408889634f;
constexpr float NEG_BIG = -3.0e38f;

using u32x4 = __attribute__((ext_vector_type(4))) unsigned;
using u32x2 = __attribute__((ext_vector_type(2))) unsigned;
using f32x4 = __attribute__((ext_vector_type(4))) float;
using f32x2 = __attribute__((ext_vector_type(2))) float;
using bf16x2 = __attribute__((ext_vector_type(2))) __bf16;
typedef unsigned long long u64;

__device__ __forceinline__ float asf(unsigned x) { return __builtin_bit_cast(float, x); }
__device__ __forceinline__ unsigned asu(float x) { return __builtin_bit_cast(unsigned, x); }
__device__ __forceinline__ float dot2(unsigned k, unsigned q, float acc) {
  return __builtin_amdgcn_fdot2_f32_bf16(__builtin_bit_cast(bf16x2, k), __builtin_bit_cast(bf16x2, q), acc, false);
}
template <int CTRL> __device__ __forceinline__ float dpp(float x) {
  return asf(__builtin_amdgcn_mov_dpp(__builtin_bit_cast(int, x), CTRL, 0xf, 0xf, true));
}
__device__ __forceinline__ float row_sum(float x) {
  x += dpp<0x128>(x); x += dpp<0x124>(x); x += dpp<0x122>(x); x += dpp<0x121>(x);
  return x;
}
__device__ __forceinline__ float row_max(float x) {
  x = fmaxf(x, dpp<0x128>(x)); x = fmaxf(x, dpp<0x124>(x)); x = fmaxf(x, dpp<0x122>(x)); x = fmaxf(x, dpp<0x121>(x));
  return x;
}
__device__ __forceinline__ float xrow_max(float x) {
  auto a = __builtin_amdgcn_permlane16_swap(asu(x), asu(x), false, false);
  x = fmaxf(asf(a[0]), asf(a[1]));
  auto c = __builtin_amdgcn_permlane32_swap(asu(x), asu(x), false, false);
  return fmaxf(asf(c[0]), asf(c[1]));
}
__device__ __forceinline__ float xrow_sum(float x) {
  auto a = __builtin_amdgcn_permlane16_swap(asu(x), asu(x), false, false);
  x = asf(a[0]) + asf(a[1]);
  auto c = __builtin_amdgcn_permlane32_swap(asu(x), asu(x), false, false);
  return asf(c[0]) + asf(c[1]);
}
__device__ __forceinline__ int clamp_len(int l) { return l < 0 ? 0 : (l > SKV ? SKV : l); }

constexpr unsigned WS_CTL = 0, WS_GM = 64 * 1024, WS_GO = 128 * 1024, WS_BYTES = WS_GO + B_ * NHQ * 8 * WGPG * 512;
constexpr unsigned OOB = 0x80000000u;

constexpr int LDS_ACC = 0, LDS_ML = NSW * 512, LDS_BT = LDS_ML + NSW * 8, LDS_FLOATS = LDS_BT + B_ * NBPS;

__global__ void __launch_bounds__(NTHR, 2) attn_decode(const unsigned short* __restrict__ q, const unsigned short* __restrict__ kc,
                                                    const unsigned short* __restrict__ vc, const int* __restrict__ bt,
                                                    const int* __restrict__ lens, unsigned char* __restrict__ ws,
                                                    unsigned short* __restrict__ out, unsigned kv_bytes) {
  __shared__ __attribute__((aligned(16))) float lds[LDS_FLOATS];
  int* lbt = reinterpret_cast<int*>(lds + LDS_BT);
  asm volatile("" :: "s"(q), "s"(kc), "s"(vc), "s"(bt), "s"(lens), "s"(ws), "s"(out), "s"(kv_bytes));
  const int tid = threadIdx.x, lane = tid & 63;
  const int wave = __builtin_amdgcn_readfirstlane(tid >> 6);
  const int hq = (blockIdx.x >> 3) & 7, j = (blockIdx.x & 7) + 8 * (blockIdx.x >> 6);

  u32x2 lr = *reinterpret_cast<const u32x2*>(lens);
  u32x4 btq[4];
#pragma unroll
  for (int i = 0; i < 4; ++i) btq[i] = reinterpret_cast<const u32x4*>(bt)[i * 64 + lane];
  __builtin_amdgcn_sched_barrier(0);
  const unsigned voff = hq * 1024 + lane * 16;
  const __amdgpu_buffer_rsrc_t wrs = __builtin_amdgcn_make_buffer_rsrc((void*)ws, 0, WS_BYTES, 0x00020000);
  const u32x4 qa = *reinterpret_cast<const u32x4*>(reinterpret_cast<const char*>(q) + voff);
  const u32x4 qb = *reinterpret_cast<const u32x4*>(reinterpret_cast<const char*>(q) + voff + H_ * D_ * 2);
  const u32x4 cw = __builtin_amdgcn_raw_buffer_load_b128(wrs, 0, WS_CTL + hq * 256u, AUX_SC1);
  const __amdgpu_buffer_rsrc_t krs = __builtin_amdgcn_make_buffer_rsrc((void*)kc, 0, kv_bytes, 0x00020000);
  const __amdgpu_buffer_rsrc_t vrs = __builtin_amdgcn_make_buffer_rsrc((void*)vc, 0, kv_bytes, 0x00020000);
  __builtin_amdgcn_sched_barrier(0);
  asm volatile("" : "+v"(lr));
  const int len0 = clamp_len(__builtin_amdgcn_readfirstlane(lr[0])), len1 = clamp_len(__builtin_amdgcn_readfirstlane(lr[1]));
  int n0 = __builtin_popcount((unsigned)__builtin_amdgcn_ballot_w64((2 * lane + 1) * (len0 + len1) <= 2 * WGPG * len0));
  n0 = min(max(n0, 1), WGPG - 1);
  const int b = j < n0 ? 0 : 1;
  const int s = b ? j - n0 : j, n = b ? WGPG - n0 : n0, len = b ? len1 : len0;
  const float rn = __builtin_amdgcn_rcpf((float)n);
  const int t0 = __builtin_amdgcn_readfirstlane((int)((float)(len * s) * rn));
  const int t1 = (s + 1 == n) ? len : __builtin_amdgcn_readfirstlane((int)((float)(len * (s + 1)) * rn));
  int tb = (t0 & ~(CH - 1)) + wave * CH;
#pragma unroll
  for (int i = 0; i < 4; ++i) reinterpret_cast<u32x4*>(lbt)[i * 64 + lane] = btq[i];
  asm volatile("" ::: "memory");
  const int* lbtb = lbt + b * NBPS;
  float m = NEG_BIG, l = 0.f;
  f32x2 acc[4];
#pragma unroll
  for (int k = 0; k < 4; ++k) acc[k] = f32x2{0.f, 0.f};
  constexpr int STEP = NSW * CH;
  const int tend = wave < NSW ? t1 : 0;
  const int tsw = tend - TAILN * STEP;
#define ISSUE(K_, V_, tb_, pg_)                                                                            \
  do {                                                                                                     \
    const unsigned so_ = (unsigned)(pg_) * PAGE_BYTES + (unsigned)((tb_) & 15) * TOK_BYTES;                \
    _Pragma("unroll") for (int u_ = 0; u_ < CH; ++u_) {                                                    \
      const unsigned o_ = ((tb_) + u_ >= t0 && (tb_) + u_ < tend) ? so_ + u_ * TOK_BYTES : kv_bytes;       \
      (K_)[u_] = __builtin_amdgcn_raw_buffer_load_b128(krs, voff, o_, AUX_NT);                             \
      (V_)[u_] = __builtin_amdgcn_raw_buffer_load_b128(vrs, voff, o_, AUX_NT);                             \
    }                                                                                                      \
  } while (0)
#define CONSUME(K_, V_)                                                                                    \
  do {                                                                                                     \
    float sco_[CH];                                                                                        \
    _Pragma("unroll") for (int u_ = 0; u_ < CH; ++u_) {                                                    \
      float d_ = (tb + u_ >= t0 && tb + u_ < tend) ? 0.f : -INFINITY;                                      \
      const u32x4 kk_ = (K_)[u_];                                                                          \
      d_ = dot2(kk_[0], qv[0], d_); d_ = dot2(kk_[1], qv[1], d_); d_ = dot2(kk_[2], qv[2], d_); d_ = dot2(kk_[3], qv[3], d_); \
      sco_[u_] = row_sum(d_);                                                                              \
    }                                                                                                      \
    float mn_ = m;                                                                                         \
    _Pragma("unroll") for (int u_ = 0; u_ < CH; ++u_) mn_ = fmaxf(mn_, sco_[u_]);                          \
    const float alpha_ = __builtin_amdgcn_exp2f((m - mn_) * C2);                                           \
    const float mc_ = -mn_ * C2;                                                                           \
    m = mn_;                                                                                               \
    l *= alpha_;                                                                                           \
    _Pragma("unroll") for (int k_ = 0; k_ < 4; ++k_) acc[k_] *= alpha_;                                    \
    _Pragma("unroll") for (int u_ = 0; u_ < CH; ++u_) {                                                    \
      const float p_ = __builtin_amdgcn_exp2f(fmaf(sco_[u_], C2, mc_));                                    \
      const u32x4 vv_ = (V_)[u_];                                                                          \
      l += p_;                                                                                             \
      _Pragma("unroll") for (int k_ = 0; k_ < 4; ++k_) {                                                   \
        const f32x2 vf_ = {asf(vv_[k_] << 16), asf(vv_[k_] & 0xffff0000u)};                               \
        acc[k_] = __builtin_elementwise_fma(f32x2{p_, p_}, vf_, acc[k_]);                                  \
      }                                                                                                    \
    }                                                                                                      \
    asm volatile("" : "+v"(acc[0]), "+v"(acc[1]), "+v"(acc[2]), "+v"(acc[3]), "+v"(l), "+v"(m));           \
  } while (0)
  u32x4 K[CH], V[CH];
  __builtin_amdgcn_sched_barrier(0);
  ISSUE(K, V, tb, __builtin_amdgcn_readfirstlane(lbtb[min(tb >> 4, NBPS - 1)]));
  __builtin_amdgcn_sched_barrier(0);
  const u64 epoch = b ? ((u64)(unsigned)__builtin_amdgcn_readfirstlane(cw[3]) << 32 | (unsigned)__builtin_amdgcn_readfirstlane(cw[2]))
                      : ((u64)(unsigned)__builtin_amdgcn_readfirstlane(cw[1]) << 32 | (unsigned)__builtin_amdgcn_readfirstlane(cw[0]));
  unsigned tag = (unsigned)(((epoch + 1) * 0x9E3779B97F4A7C15ull) >> 32);
  asm volatile("" : "+s"(tag));
  const u32x4 qv = b ? qb : qa;
  for (;;) {
    const int pgv = lbtb[min((tb + STEP) >> 4, NBPS - 1)];
    CONSUME(K, V);
    __builtin_amdgcn_sched_barrier(0);
    tb += STEP;
    asm volatile("" : "+s"(tb));
    if (tb >= tsw) break;
    ISSUE(K, V, tb, __builtin_amdgcn_readfirstlane(pgv));
    __builtin_amdgcn_sched_barrier(0);
  }
  {
    u32x4 KB[TAILN * CH], VB[TAILN * CH];
    int pgs[TAILN];
#pragma unroll
    for (int c = 0; c < TAILN; ++c) pgs[c] = lbtb[min((tb + c * STEP) >> 4, NBPS - 1)];
#pragma unroll
    for (int c = 0; c < TAILN; ++c) ISSUE(KB + c * CH, VB + c * CH, tb + c * STEP, __builtin_amdgcn_readfirstlane(pgs[c]));
    __builtin_amdgcn_sched_barrier(0);
#pragma unroll
    for (int c = 0; c < TAILN; ++c) {
      CONSUME(KB + c * CH, VB + c * CH);
      __builtin_amdgcn_sched_barrier(0);
      tb += STEP;
    }
  }
#undef ISSUE
#undef CONSUME
  const int grp = b * NHQ + hq;
  float* lacc = lds + LDS_ACC;
  float* lml = lds + LDS_ML;
  if (wave < NSW) {
    *reinterpret_cast<f32x4*>(lacc + (wave * 64 + lane) * 8) = f32x4{acc[0][0], acc[0][1], acc[1][0], acc[1][1]};
    *reinterpret_cast<f32x4*>(lacc + (wave * 64 + lane) * 8 + 4) = f32x4{acc[2][0], acc[2][1], acc[3][0], acc[3][1]};
    if ((lane & 15) == 0) *reinterpret_cast<f32x2*>(lml + (wave * 4 + (lane >> 4)) * 2) = f32x2{m, l};
  }
  asm volatile("s_waitcnt lgkmcnt(0)\n\ts_barrier" ::: "memory");
  if (wave < 4) {
    f32x2 ml[NSW];
    float x0[NSW], x1[NSW];
#pragma unroll
    for (int w = 0; w < NSW; ++w) {
      ml[w] = *reinterpret_cast<const f32x2*>(lml + (w * 4 + wave) * 2);
      x0[w] = lacc[(w * 64 + wave * 16) * 8 + lane];
      x1[w] = lacc[(w * 64 + wave * 16) * 8 + 64 + lane];
    }
    float M = ml[0][0];
#pragma unroll
    for (int w = 1; w < NSW; ++w) M = fmaxf(M, ml[w][0]);
    float L = 0.f, o0 = 0.f, o1 = 0.f;
#pragma unroll
    for (int w = 0; w < NSW; ++w) {
      const float wgt = __builtin_amdgcn_exp2f((ml[w][0] - M) * C2);
      L = fmaf(wgt, ml[w][1], L); o0 = fmaf(wgt, x0[w], o0); o1 = fmaf(wgt, x1[w], o1);
    }
    const unsigned gso = WS_GO + (unsigned)((grp * 8 + wave * 2) * WGPG + s) * 512u;
    __builtin_amdgcn_raw_buffer_store_b64(u32x2{asu(o0), tag}, wrs, lane * 8, gso, AUX_SC1);
    __builtin_amdgcn_raw_buffer_store_b64(u32x2{asu(o1), tag}, wrs, lane * 8, gso + WGPG * 512u, AUX_SC1);
    __builtin_amdgcn_raw_buffer_store_b64(u32x2{asu((lane & 1) ? L : M), tag}, wrs, lane < 2 ? lane * 8 : OOB,
                                          WS_GM + (unsigned)((grp * 4 + wave) * WGPG + s) * 16u, AUX_SC1);
    return;
  }
  const int g = wave - 4, r = lane >> 4, c = lane & 15;
  const int msrc = (c & 7) * 4 + r;
  const unsigned moff = msrc * 16 + (c >> 3) * 8, mwant = msrc < n ? tag : 0u;
  const unsigned ooff = r * 512 + (g * 16 + c) * 8;
  unsigned want[8];
#pragma unroll
  for (int i = 0; i < 8; ++i) want[i] = 4 * i + r < n ? tag : 0u;
#pragma nounroll
  for (int k = s; k < 8; k += n) {
    const int hr = k >> 1, half = k & 1;
    const __amdgpu_buffer_rsrc_t mrs = __builtin_amdgcn_make_buffer_rsrc(ws + WS_GM + (unsigned)((grp * 4 + hr) * WGPG) * 16u, 0, n * 16, 0x00020000);
    unsigned char* obase = ws + WS_GO + (unsigned)((grp * 8 + k) * WGPG) * 512u;
    u32x2 gm, go[8];
    bool full = true;
#pragma nounroll
    for (int spin = 0; spin < SPIN_CAP; ++spin) {
      asm volatile("" ::: "memory");
      const __amdgpu_buffer_rsrc_t grs = __builtin_amdgcn_make_buffer_rsrc(obase, 0, full ? n * 512 : 0, 0x00020000);
      gm = __builtin_amdgcn_raw_buffer_load_b64(mrs, moff, 0, AUX_SC1);
#pragma unroll
      for (int i = 0; i < 8; ++i) go[i] = __builtin_amdgcn_raw_buffer_load_b64(grs, ooff, i * 2048, AUX_SC1);
      const int miss = __builtin_popcountll(__builtin_amdgcn_ballot_w64(gm[1] != mwant));
      unsigned bad = 0u;
#pragma unroll
      for (int i = 0; i < 8; ++i) bad |= go[i][1] ^ want[i];
      if (full && miss == 0 && __builtin_amdgcn_ballot_w64(bad != 0u) == 0ull) break;
      full = miss <= n;
      if (!full) __builtin_amdgcn_s_sleep(4);
    }
    const float mv = (c < 8 && msrc < n) ? asf(gm[0]) : NEG_BIG;
    const float GM = xrow_max(row_max(mv));
    const float wv = __builtin_amdgcn_exp2f((mv - GM) * C2);
    float o = dpp<0x150>(wv) * asf(go[0][0]);
    o = fmaf(dpp<0x151>(wv), asf(go[1][0]), o); o = fmaf(dpp<0x152>(wv), asf(go[2][0]), o); o = fmaf(dpp<0x153>(wv), asf(go[3][0]), o);
    o = fmaf(dpp<0x154>(wv), asf(go[4][0]), o); o = fmaf(dpp<0x155>(wv), asf(go[5][0]), o); o = fmaf(dpp<0x156>(wv), asf(go[6][0]), o);
    o = fmaf(dpp<0x157>(wv), asf(go[7][0]), o);
    const float wl = dpp<0x128>(wv);
    float GL = xrow_sum(row_sum(c >= 8 ? wl * asf(gm[0]) : 0.f));
    float GO = xrow_sum(o);
    const int head = hq * 4 + hr, d = half * 64 + g * 16 + c;
    constexpr float MASK_RAW = -998244352.0f / SCALE;
    if (len < SKV && (GM - MASK_RAW) * C2 < 160.f) {
      const float M2 = fmaxf(GM, MASK_RAW);
      const float w0 = (GL > 0.f) ? __builtin_amdgcn_exp2f((GM - M2) * C2) : 0.f;
      const float wm = __builtin_amdgcn_exp2f((MASK_RAW - M2) * C2);
      float sv = 0.f;
      for (int t = len + r; t < SKV; t += 4)
        sv += asf((unsigned)vc[(((size_t)lbtb[t >> 4] * PAGE + (t & 15)) * H_ + head) * D_ + d] << 16);
      sv = xrow_sum(sv);
      GL = GL * w0 + wm * (float)(SKV - len);
      GO = GO * w0 + wm * sv;
    }
    if (r == 0) out[(b * H_ + head) * D_ + d] = __builtin_bit_cast(unsigned short, (__bf16)(GO * __builtin_amdgcn_rcpf(GL)));
    if (k == 0 && g == 0)
      __builtin_amdgcn_raw_buffer_store_b64(u32x2{(unsigned)(epoch + 1), (unsigned)((epoch + 1) >> 32)}, wrs,
                                            lane == 0 ? b * 8u : OOB, WS_CTL + hq * 256u, AUX_SC1);
  }
}

extern "C" void kernel_launch(void* const* d_in, const int* in_sizes, int n_in, void* d_out, int out_size, void* d_ws,
                              size_t ws_size, hipStream_t stream) {
  const unsigned short* q = (const unsigned short*)d_in[0];
  const unsigned short* k = (const unsigned short*)d_in[1];
  const unsigned short* v = (const unsigned short*)d_in[2];
  const int* bt = (const int*)d_in[3];
  const int* lens = (const int*)d_in[4];
  const unsigned kv_bytes = (unsigned)in_sizes[1] * 2u;
  attn_decode<<<NWG, NTHR, 0, stream>>>(q, k, v, bt, lens, (unsigned char*)d_ws, (unsigned short*)d_out, kv_bytes);
}
```
